```python
import torch

def _softmax(x, axis=-1):
    return torch.softmax(x, dim=axis)

def _gelu(x):
    return torch.nn.functional.gelu(x, approximate='tanh')

def _silu(x):
    return torch.nn.functional.silu(x)

def _sigmoid(x):
    return torch.sigmoid(x)

def _layernorm(x, axis=-1, eps=1e-05):
    m = x.mean(dim=axis, keepdim=True)
    v = x.var(dim=axis, keepdim=True, unbiased=False)
    return (x - m) / torch.sqrt(v + eps)

def _scatter_add(tgt, idx, src, axis=0):
    return tgt.clone().index_add_(axis, idx, src)

def _segment_sum(src, seg, n):
    out = torch.zeros((n,) + src.shape[1:], dtype=src.dtype)
    return out.index_add_(0, seg, src)

def _pad(x, pw):
    flat = tuple((v for pair in reversed(pw) for v in pair))
    return torch.nn.functional.pad(x, flat)
kernel_name = 'standalone__tree_speculative_verify_tree_attention_over_candidate_token_tree__sampling'

def setup_inputs():

    _w = lambda a: (lambda t: t.to(torch.bfloat16) if t.is_floating_point() else t)(torch.as_tensor(a))
    B, Hq, Hkv, T, C, D = (8, 32, 8, 1, 8192, 128)
    tree_mask = torch.zeros((B, T, T), dtype=float)
    for b in range(B):
        parent = torch.zeros(T, dtype=torch.int64)
        for i in range(1, T):
            parent[i] = int(torch.randint(0, i, (1,)).item())
        for i in range(T):
            j = i
            tree_mask[b, i, j] = 1.0
            while j > 0:
                j = parent[j]
                tree_mask[b, i, j] = 1.0
    return {'q': _w((lambda *s: torch.randn(*s, dtype=torch.bfloat16) * 0.8)(B, Hq, T, D)), 'k_cache': _w((lambda *s: torch.randn(*s, dtype=torch.bfloat16) * 0.8)(B, Hkv, C, D)), 'v_cache': _w((lambda *s: torch.randn(*s, dtype=torch.bfloat16) * 0.05)(B, Hkv, C, D)), 'k_tree': _w((lambda *s: torch.randn(*s, dtype=torch.bfloat16) * 0.8)(B, Hkv, T, D)), 'v_tree': _w((lambda *s: torch.randn(*s, dtype=torch.bfloat16) * 0.05)(B, Hkv, T, D)), 'tree_mask': _w(tree_mask)}

def reference(q, k_cache, v_cache, k_tree, v_tree, tree_mask):
    B, Hq, T, D = q.shape
    Hkv, C = (k_cache.shape[1], k_cache.shape[2])
    rep = Hq // Hkv
    kc = (lambda x, n, axis: torch.repeat_interleave(x, n, dim=axis))(k_cache, rep, axis=1)
    vc = (lambda x, n, axis: torch.repeat_interleave(x, n, dim=axis))(v_cache, rep, axis=1)
    kt = (lambda x, n, axis: torch.repeat_interleave(x, n, dim=axis))(k_tree, rep, axis=1)
    vt = (lambda x, n, axis: torch.repeat_interleave(x, n, dim=axis))(v_tree, rep, axis=1)
    k = torch.concatenate([kc, kt], axis=2)
    v = torch.concatenate([vc, vt], axis=2)
    scale = D ** (-0.5)
    scores = torch.einsum('bhqd,bhkd->bhqk', q, k) * scale
    ctx_mask = torch.ones((B, 1, T, C), dtype=scores.dtype)
    tm = tree_mask[:, None, :, :].clone()
    full_mask = torch.concatenate([ctx_mask, tm], axis=-1)
    scores = scores + (1.0 - full_mask) * -1000000000.0
    p = _softmax(scores, axis=-1)
    return torch.einsum('bhqk,bhkd->bhqd', p, v)
```

```cpp
#define P_NW 8
#define P_W 4
#define P_SG 2
#define P_MAP 0
#include <hip/hip_runtime.h>
#include <hip/hip_bf16.h>

#define P_NS 4

namespace {
constexpr int B_ = 8, HQ = 32, HKV = 8, CTX = 8192, D = 128, G = HQ / HKV;
constexpr int NBH = B_ * HKV;
constexpr int NW = P_NW, NS = P_NS, W = P_W, SG = P_SG;
constexpr int NWG = NBH * NS;
constexpr int NH = CTX / 4 / (NS * NW);
constexpr int PH = 32 * SG;
constexpr int NPH = NH / PH;
constexpr int N = 2 * NH;
static_assert(NH % PH == 0, "");
constexpr float C2 = 0.088388347648318440f * 1.4426950408889634f;

typedef unsigned u32x4 __attribute__((ext_vector_type(4)));
typedef float f32x4 __attribute__((ext_vector_type(4)));
typedef float f32x2 __attribute__((ext_vector_type(2)));
typedef short bf16x8 __attribute__((ext_vector_type(8)));
typedef __bf16 bf16x8_t __attribute__((ext_vector_type(8)));
typedef __bf16 bf16x2_t __attribute__((ext_vector_type(2)));

__device__ __forceinline__ f32x4 mfma16(u32x4 a, u32x4 b, f32x4 c) {
  return __builtin_amdgcn_mfma_f32_16x16x32_bf16(__builtin_bit_cast(bf16x8_t, a), __builtin_bit_cast(bf16x8_t, b), c, 0, 0, 0);
}
template <int CTRL> __device__ __forceinline__ float dpp(float x) {
  return __builtin_bit_cast(float, __builtin_amdgcn_mov_dpp(__builtin_bit_cast(int, x), CTRL, 0xf, 0xf, true));
}
template <int BANK> __device__ __forceinline__ float dpp_bank(float old, float x) {
  return __builtin_bit_cast(float, __builtin_amdgcn_update_dpp(__builtin_bit_cast(int, old), __builtin_bit_cast(int, x), 0xE4, 0xf, 1 << BANK, false));
}
constexpr int ROR4 = 0x124, ROR8 = 0x128, ROR12 = 0x12C;
__device__ __forceinline__ float xrow_max(float x) {
  auto s = __builtin_amdgcn_permlane16_swap(__float_as_uint(x), __float_as_uint(x), false, false);
  x = fmaxf(__uint_as_float(s[0]), __uint_as_float(s[1]));
  auto t = __builtin_amdgcn_permlane32_swap(__float_as_uint(x), __float_as_uint(x), false, false);
  return fmaxf(__uint_as_float(t[0]), __uint_as_float(t[1]));
}
__device__ __forceinline__ float xrow_sum(float x) {
  auto s = __builtin_amdgcn_permlane16_swap(__float_as_uint(x), __float_as_uint(x), false, false);
  x = __uint_as_float(s[0]) + __uint_as_float(s[1]);
  auto t = __builtin_amdgcn_permlane32_swap(__float_as_uint(x), __float_as_uint(x), false, false);
  return __uint_as_float(t[0]) + __uint_as_float(t[1]);
}
__device__ __forceinline__ unsigned pack_bf16(float lo, float hi) {
  bf16x2_t v = {(__bf16)lo, (__bf16)hi};
  return __builtin_bit_cast(unsigned, v);
}
template <int I> __device__ __forceinline__ float score_sum(f32x4 d) {
  float t = d[I];
  t += dpp<0x120 + 4 * 1>(d[(I + 3) & 3]);
  t += dpp<0x120 + 4 * 2>(d[(I + 2) & 3]);
  t += dpp<0x120 + 4 * 3>(d[(I + 1) & 3]);
  return t;
}

template <int I> struct IC { static constexpr int value = I; };
template <int I, int E, typename F> __device__ __forceinline__ void static_for(F&& f) {
  if constexpr (I < E) { f(IC<I>{}); static_for<I + 1, E>(f); }
}

__global__ void __launch_bounds__(NW * 64) attn_split(const unsigned short* __restrict__ q, const unsigned short* __restrict__ kc,
                                                      const unsigned short* __restrict__ vc, float* __restrict__ po, float* __restrict__ pml) {
  __shared__ __attribute__((aligned(16))) float lds[NW * (G * D + 8)];
  const int tid = threadIdx.x, lane = tid & 63;
  const int wave = __builtin_amdgcn_readfirstlane(tid >> 6);
  const int bid = blockIdx.x;
  const int s = P_MAP == 0 ? bid % NS : bid / NBH, bh = P_MAP == 0 ? bid / NS : bid % NBH;
  constexpr unsigned WG_BYTES = CTX * D * 2 / NS;
  const size_t wgoff = (size_t)bh * (CTX * D * 2) + (size_t)s * WG_BYTES;
  const auto rk = __builtin_amdgcn_make_buffer_rsrc((void*)((const char*)kc + wgoff), 0, WG_BYTES, 0x00020000);
  const auto rv = __builtin_amdgcn_make_buffer_rsrc((void*)((const char*)vc + wgoff), 0, WG_BYTES, 0x00020000);
  const int voffk = ((lane >> 2) & 3) * 256 + ((lane >> 4) * 4 + (lane & 3)) * 16;
  const int voffv = lane * 16;
  const unsigned wbase = wave * 4096;
  auto ld = [&](int i) -> u32x4 {
    const int blk = i / PH, r = i % PH;
    const int isv = blk & 1, n = (blk >> 1) * PH + r;
    const unsigned soff = wbase + (n >> 2) * (NW * 4096);
    return __builtin_amdgcn_raw_buffer_load_b128(isv ? rv : rk, (isv ? voffv : voffk) + (n & 3) * 1024, soff, 2);
  };
  const u32x4 qf = *reinterpret_cast<const u32x4*>(q + ((size_t)bh * G + (lane & 3)) * D + ((lane >> 4) * 4 + ((lane >> 2) & 3)) * 8);
  __builtin_amdgcn_sched_barrier(0);
  u32x4 buf[W];
#pragma unroll
  for (int j = 0; j < W; ++j) buf[j] = ld(j);
  unsigned bmask[4];
#pragma unroll
  for (int i = 0; i < 4; ++i) bmask[i] = (((lane >> 2) & 3) == i) ? 0xffffffffu : 0u;
  __builtin_amdgcn_sched_barrier(0);

  f32x4 acc[8];
#pragma unroll
  for (int j = 0; j < 8; ++j) acc[j] = f32x4{0.f, 0.f, 0.f, 0.f};
  float m_run = -INFINITY, l_run = 0.f;
  float x[8 * SG];
  unsigned pk[4 * SG];
  u32x4 bv[8];
  static_for<0, N>([&](auto ic) __attribute__((always_inline)) {
    constexpr int i = decltype(ic)::value;
    constexpr int blk = i / PH, r = i % PH, isv = blk & 1;
    if constexpr (!isv) {
      const f32x4 d = mfma16(buf[i % W], qf, f32x4{0.f, 0.f, 0.f, 0.f});
      if constexpr (i + W < N) buf[i % W] = ld(i + W);
      constexpr int reg = (r >> 5) * 8 + (r & 7), bank = (r >> 3) & 3;
      if constexpr (bank == 0) x[reg] = score_sum<0>(d);
      else x[reg] = dpp_bank<bank>(x[reg], score_sum<bank>(d));
      if constexpr (r == PH - 1) {
        float mx = x[0];
#pragma unroll
        for (int u = 1; u < 8 * SG; ++u) mx = fmaxf(mx, x[u]);
        mx = fmaxf(mx, dpp<ROR4>(mx)); mx = fmaxf(mx, dpp<ROR8>(mx)); mx = xrow_max(mx);
        const float mn = fmaxf(m_run, mx * C2);
        if constexpr (blk > 0) {
          const float alpha = __builtin_amdgcn_exp2f(m_run - mn);
          l_run *= alpha;
#pragma unroll
          for (int h = 0; h < 4; ++h) { const float ah = __builtin_bit_cast(float, __builtin_amdgcn_readlane(__builtin_bit_cast(int, alpha), h));
#pragma unroll
            for (int j = 0; j < 8; ++j) acc[j][h] *= ah; }
        }
        m_run = mn;
#pragma unroll
        for (int u = 0; u < 8 * SG; ++u) { x[u] = __builtin_amdgcn_exp2f(fmaf(x[u], C2, -mn)); l_run += x[u]; }
#pragma unroll
        for (int u = 0; u < 4 * SG; ++u) pk[u] = pack_bf16(x[2 * u], x[2 * u + 1]);
      }
    } else if constexpr (r & 1) {
      const u32x4 r0 = buf[(i - 1) % W], r1 = buf[i % W];
      constexpr int w = (r & 7) >> 1;
#pragma unroll
      for (int j = 0; j < 8; ++j) bv[j][w] = __builtin_amdgcn_perm(r1[j >> 1], r0[j >> 1], (j & 1) ? 0x07060302u : 0x05040100u);
      if constexpr (i - 1 + W < N) buf[(i - 1) % W] = ld(i - 1 + W);
      if constexpr (i + W < N) buf[i % W] = ld(i + W);
      if constexpr ((r & 7) == 7) {
        constexpr int sg = r >> 5, bank = (r >> 3) & 3;
        const u32x4 a = {pk[4 * sg] & bmask[bank], pk[4 * sg + 1] & bmask[bank], pk[4 * sg + 2] & bmask[bank], pk[4 * sg + 3] & bmask[bank]};
#pragma unroll
        for (int j = 0; j < 8; ++j) acc[j] = mfma16(a, bv[j], acc[j]);
      }
    }
    __builtin_amdgcn_sched_barrier(0);
  });
  l_run += dpp<ROR4>(l_run); l_run += dpp<ROR8>(l_run); l_run = xrow_sum(l_run);
  float* rec = lds + wave * (G * D + 8);
#pragma unroll
  for (int h = 0; h < 4; ++h) {
    float y[4];
#pragma unroll
    for (int j = 0; j < 4; ++j) { auto t = __builtin_amdgcn_permlane32_swap(__float_as_uint(acc[j][h]), __float_as_uint(acc[j + 4][h]), false, false); y[j] = __uint_as_float(t[0]) + __uint_as_float(t[1]); }
    f32x2 z;
#pragma unroll
    for (int j = 0; j < 2; ++j) { auto t = __builtin_amdgcn_permlane16_swap(__float_as_uint(y[j]), __float_as_uint(y[j + 2]), false, false); z[j] = __uint_as_float(t[0]) + __uint_as_float(t[1]); }
    *reinterpret_cast<f32x2*>(rec + h * D + 8 * (lane & 15) + 2 * (lane >> 4)) = z;
  }
  if (lane < 4) { rec[G * D + lane] = m_run; rec[G * D + 4 + lane] = l_run; }
  __syncthreads();
  for (int e = tid; e < G * D; e += NW * 64) {
    const int h = e >> 7;
    float M = -INFINITY;
#pragma unroll
    for (int w = 0; w < NW; ++w) M = fmaxf(M, lds[w * (G * D + 8) + G * D + h]);
    float L = 0.f, o = 0.f;
#pragma unroll
    for (int w = 0; w < NW; ++w) { const float* rw = lds + w * (G * D + 8); const float sc = __builtin_amdgcn_exp2f(rw[G * D + h] - M); L = fmaf(sc, rw[G * D + 4 + h], L); o = fmaf(sc, rw[e], o); }
    po[((size_t)bh * NS + s) * (G * D) + e] = o;
    if ((e & 127) == 0) { pml[(((size_t)bh * NS + s) * G + h) * 2] = M; pml[(((size_t)bh * NS + s) * G + h) * 2 + 1] = L; }
  }
}

__device__ __forceinline__ float bf2f(unsigned short u) { return __uint_as_float((unsigned)u << 16); }

__global__ void __launch_bounds__(64) attn_combine(const unsigned short* __restrict__ q, const unsigned short* __restrict__ kt, const unsigned short* __restrict__ vt,
                                                   const unsigned short* __restrict__ tm, const float* __restrict__ po, const float* __restrict__ pml, unsigned* __restrict__ out) {
  const int row = blockIdx.x, bh = row >> 2, h = row & 3, lane = threadIdx.x;
  const unsigned qq = reinterpret_cast<const unsigned*>(q)[row * 64 + lane];
  const unsigned kk = reinterpret_cast<const unsigned*>(kt)[bh * 64 + lane];
  const unsigned vv = reinterpret_cast<const unsigned*>(vt)[bh * 64 + lane];
  const float mask = bf2f(tm[bh >> 3]);
  f32x2 ml[NS], o[NS];
#pragma unroll
  for (int s = 0; s < NS; ++s) { ml[s] = *reinterpret_cast<const f32x2*>(pml + (((size_t)bh * NS + s) * G + h) * 2); o[s] = *reinterpret_cast<const f32x2*>(po + ((size_t)bh * NS + s) * (G * D) + h * D + 2 * lane); }
  float dot = __uint_as_float(qq << 16) * __uint_as_float(kk << 16) + __uint_as_float(qq & 0xffff0000u) * __uint_as_float(kk & 0xffff0000u);
  dot += dpp<0xB1>(dot); dot += dpp<0x4E>(dot); dot += dpp<ROR4>(dot); dot += dpp<ROR8>(dot); dot = xrow_sum(dot);
  const float st = (dot * 0.088388347648318440f + (1.0f - mask) * -1000000000.0f) * 1.4426950408889634f;
  float M = st;
#pragma unroll
  for (int s = 0; s < NS; ++s) M = fmaxf(M, ml[s][0]);
  const float pt = __builtin_amdgcn_exp2f(st - M);
  float L = pt, o0 = pt * __uint_as_float(vv << 16), o1 = pt * __uint_as_float(vv & 0xffff0000u);
#pragma unroll
  for (int s = 0; s < NS; ++s) { const float sc = __builtin_amdgcn_exp2f(ml[s][0] - M); L = fmaf(sc, ml[s][1], L); o0 = fmaf(sc, o[s][0], o0); o1 = fmaf(sc, o[s][1], o1); }
  const float inv = 1.0f / L;
  out[row * 64 + lane] = pack_bf16(o0 * inv, o1 * inv);
}
}

extern "C" void kernel_launch(void* const* d_in, const int* in_sizes, int n_in, void* d_out, int out_size, void* d_ws, size_t ws_size, hipStream_t stream) {
  const unsigned short* q = (const unsigned short*)d_in[0];
  const unsigned short* kc = (const unsigned short*)d_in[1];
  const unsigned short* vc = (const unsigned short*)d_in[2];
  const unsigned short* kt = (const unsigned short*)d_in[3];
  const unsigned short* vt = (const unsigned short*)d_in[4];
  const unsigned short* tm = (const unsigned short*)d_in[5];
  float* po = (float*)d_ws;
  float* pml = po + (size_t)NWG * G * D;
  attn_split<<<NWG, NW * 64, 0, stream>>>(q, kc, vc, po, pml);
  attn_combine<<<B_ * HQ, 64, 0, stream>>>(q, kt, vt, tm, po, pml, (unsigned*)d_out);
}
```

```cpp
#include <hip/hip_runtime.h>
#include <hip/hip_bf16.h>

#define P_PACE 3470
#define P_OFF 95

namespace {
constexpr int B_ = 8, HQ = 32, HKV = 8, CTX = 8192, D = 128, G = HQ / HKV;
constexpr int NW = 8, NS = 4, NWG = B_ * HKV * NS, NT = NW * 64;
constexpr int NIT = CTX / 32 / (NS * NW);
constexpr float SCALE = 0.088388347648318440f, LOG2E = 1.4426950408889634f, C2 = SCALE * LOG2E;
static_assert(NIT == 8 && NS == G, "layout");

typedef unsigned u32x4 __attribute__((ext_vector_type(4)));
typedef unsigned u32x2 __attribute__((ext_vector_type(2)));
typedef unsigned long long u64x8 __attribute__((ext_vector_type(8)));
typedef float f32x4 __attribute__((ext_vector_type(4)));
typedef float f32x2 __attribute__((ext_vector_type(2)));
typedef __bf16 bf16x8_t __attribute__((ext_vector_type(8)));
typedef __bf16 bf16x2_t __attribute__((ext_vector_type(2)));
#define GAS __attribute__((address_space(1)))
constexpr int AUX_NT = 2, AUX_SC1 = 16;

__device__ __forceinline__ f32x4 mfma16(u32x4 a, u32x4 b, f32x4 c) {
  return __builtin_amdgcn_mfma_f32_16x16x32_bf16(__builtin_bit_cast(bf16x8_t, a), __builtin_bit_cast(bf16x8_t, b), c, 0, 0, 0);
}
template <int CTRL> __device__ __forceinline__ float dpp(float x) {
  return __builtin_bit_cast(float, __builtin_amdgcn_mov_dpp(__builtin_bit_cast(int, x), CTRL, 0xf, 0xf, true));
}
template <int BANK> __device__ __forceinline__ float dpp_bank(float old, float x) {
  return __builtin_bit_cast(float, __builtin_amdgcn_update_dpp(__builtin_bit_cast(int, old), __builtin_bit_cast(int, x), 0xE4, 0xf, 1 << BANK, false));
}
constexpr int ROR4 = 0x124, ROR8 = 0x128, ROR12 = 0x12C;
__device__ __forceinline__ float xrow_max(float x) {
  auto s = __builtin_amdgcn_permlane16_swap(__float_as_uint(x), __float_as_uint(x), false, false);
  x = fmaxf(__uint_as_float(s[0]), __uint_as_float(s[1]));
  auto t = __builtin_amdgcn_permlane32_swap(__float_as_uint(x), __float_as_uint(x), false, false);
  return fmaxf(__uint_as_float(t[0]), __uint_as_float(t[1]));
}
__device__ __forceinline__ float xrow_sum(float x) {
  auto s = __builtin_amdgcn_permlane16_swap(__float_as_uint(x), __float_as_uint(x), false, false);
  x = __uint_as_float(s[0]) + __uint_as_float(s[1]);
  auto t = __builtin_amdgcn_permlane32_swap(__float_as_uint(x), __float_as_uint(x), false, false);
  return __uint_as_float(t[0]) + __uint_as_float(t[1]);
}
__device__ __forceinline__ unsigned pack_bf16(float lo, float hi) {
  bf16x2_t v = {(__bf16)lo, (__bf16)hi};
  return __builtin_bit_cast(unsigned, v);
}
template <int I> __device__ __forceinline__ float score_sum(f32x4 d) {
  float t = d[I];
  t += dpp<ROR4>(d[(I + 3) & 3]);
  t += dpp<ROR8>(d[(I + 2) & 3]);
  t += dpp<ROR12>(d[(I + 1) & 3]);
  return t;
}
template <int I> struct IC { static constexpr int value = I; };
template <int I, int E, typename F> __device__ __forceinline__ void static_for(F&& f) {
  if constexpr (I < E) { f(IC<I>{}); static_for<I + 1, E>(f); }
}
__device__ __forceinline__ auto make_rsrc(const void* p, unsigned bytes) { return __builtin_amdgcn_make_buffer_rsrc((void*)p, 0, bytes, 0x00020000); }

constexpr unsigned WS_HEADS = 1024, HSTR = 1024 + NS * NS * 1024, WS_END = WS_HEADS + B_ * HKV * HSTR;
constexpr int WLDS = 1536, L_ML = NW * WLDS, L_CNT = L_ML + (NW + 1) * 8, LDS_N = L_CNT + 4;
constexpr unsigned SPIN_CAP = 1u << 18;
constexpr unsigned PER16 = P_PACE / 2;
constexpr unsigned WG_BYTES = CTX * D * 2 / NS;

__global__ void __launch_bounds__(NT, 2) attn_kernel(const void* q_, const void* kc_, const void* vc_, const void* kt_, const void* vt_,
                                                     const void* tm_, void* out_, void* ws_) {
  __shared__ __attribute__((aligned(16))) float lds[LDS_N];
  u64x8 ka;
  unsigned long long pc; unsigned toff;
  const int tid = threadIdx.x;
  const unsigned bid = blockIdx.x;
  asm volatile("s_load_dwordx16 %0, %1, 0x0" : "=s"(ka) : "s"(__builtin_amdgcn_kernarg_segment_ptr()));
  __builtin_amdgcn_sched_barrier(0);
  unsigned tx = (tid << 6) ^ (bid << 9);
  asm volatile("s_getpc_b64 %0\n.Lattn_code_here:\n\tv_min_u32 %1, .Lattn_code_tail-.Lattn_code_here-4, %2" : "=s"(pc), "=&v"(toff), "+v"(tx));
  const unsigned ctouch = *(const GAS unsigned*)((const GAS char*)pc + toff);
  int voffk = (tx & 0x7300) | ((tid << 4) & 0x30) | ((tid << 2) & 0xc0);
  unsigned koff = bid * WG_BYTES + voffk;
  asm volatile("" : "+v"(koff));
  __builtin_amdgcn_sched_barrier(0);
  asm volatile("s_waitcnt lgkmcnt(0)" : "+s"(ka));
  u32x4 buf[4];
#pragma unroll
  for (int n = 0; n < 4; ++n) buf[n] = __builtin_nontemporal_load((const GAS u32x4*)((const GAS char*)ka[1] + koff + n * 1024));
  __builtin_amdgcn_sched_barrier(0);
  const unsigned long long t0 = __builtin_amdgcn_s_memrealtime();
  __builtin_amdgcn_sched_barrier(0);
  const int lane = tid & 63;
  const int wave = __builtin_amdgcn_readfirstlane(tid >> 6);
  const unsigned s = bid & 3, bh = bid >> 2;
  const u32x4 qf = *(const GAS u32x4*)((const GAS char*)ka[0] + (unsigned)(((bh * G + (lane & 3)) * D + (lane >> 2) * 8) * 2));
  __builtin_amdgcn_sched_barrier(0);
  const char* kb = (const char*)ka[1] + (size_t)bid * WG_BYTES;
  const char* vb = (const char*)ka[2] + (size_t)bid * WG_BYTES;
  unsigned char* ws = (unsigned char*)ka[7];
  const auto rk = make_rsrc(kb, WG_BYTES), rv = make_rsrc(vb, WG_BYTES);
  const int voffv = ((tx & 0x7000) | (lane * 16)) ^ ((bid & 1) << 9);
  unsigned char* wsh = ws + WS_HEADS + bh * HSTR;
  const auto rw = make_rsrc(wsh, HSTR);
  const unsigned t016 = ((unsigned)t0 & 0x7ffffffu) * 16u;
  const unsigned rot = (__builtin_popcount(bid & 0x63) & 1) | ((__builtin_popcount(bid & 0xb6) & 1) << 1) | ((__builtin_popcount(bid & 0xca) & 1) << 2);
  const unsigned ph = (wave ^ rot ^ ((bid >> 3) & 7)) * (PER16 / NW) + (((bid & 3) * 2) | ((bid >> 3) & 24)) * (PER16 / 256);
  auto first_tick = [&](int origin) -> unsigned { const unsigned t = t016 + origin + (P_OFF * 16 + PER16 - 1); return t - (t + 64 * PER16 - ph) % PER16; };
  unsigned tnext = first_tick(0), delta = 0;
  const auto rs = make_rsrc(ws, WS_END);
  if ((unsigned)wave == (bid >> 5) && (bid & 24) == 0 && lane == 0)
    __builtin_amdgcn_raw_buffer_store_b64(u32x2{(unsigned)t0, (unsigned)(t0 >> 32)}, rs, 0, (bid >> 5) * 64 + (bid & 7) * 8, AUX_SC1);
  const int gw = 1 + (int)(((bid >> 3) * 5 + wave * 3) % 12);
#define PACE() do { while ((int)(((((unsigned)__builtin_amdgcn_s_memrealtime() & 0x7ffffffu) * 16u) - tnext) << 1) < 0) __builtin_amdgcn_s_sleep(1); \
                    tnext += PER16; __builtin_amdgcn_sched_barrier(0); } while (0)
  if (tid == 0) reinterpret_cast<unsigned*>(lds)[L_CNT] = 0u;
  __syncthreads();
  unsigned tag = 0;
  const u32x4 ktf = __builtin_amdgcn_raw_buffer_load_b128(make_rsrc((const char*)ka[3] + bh * (D * 2), (s | wave) == 0 ? D * 2 : 0), voffk & 0xf0, 0, 0);
  const unsigned vtu = __builtin_amdgcn_raw_buffer_load_b32(make_rsrc((const char*)ka[4] + bh * (D * 2), (s | (wave >> 2)) == 0 ? D * 2 : 0), lane * 4, 0, 0);
  const unsigned tmw = __builtin_amdgcn_raw_buffer_load_b32(make_rsrc((const void*)ka[5], (s | wave) == 0 ? B_ * 2 : 0), 0, (bh >> 4) * 4, 0);
  const unsigned vtouch = __builtin_amdgcn_raw_buffer_load_b32(rv, 0, (unsigned)(wave ^ 1) * WG_BYTES, AUX_NT);
  float* wl = lds + wave * WLDS;
  float mx = -INFINITY;
  __builtin_amdgcn_sched_barrier(0);

#pragma clang loop unroll(disable)
  for (int it = 0; it < NIT; ++it) {
    const bool last = it == NIT - 1;
    const unsigned base = it * 65536, nbase = last ? 0 : base + 65536;
    const auto rn = make_rsrc(last ? vb : kb, WG_BYTES);
    const int voffn = last ? voffv : voffk;
    unsigned smoff = it == 3 ? WS_HEADS + bh * HSTR : WS_END;
    smoff = it == 1 ? 0 : smoff;
    asm volatile("" : "+s"(smoff));
    f32x2 x;
    static_for<0, 2>([&](auto hc) __attribute__((always_inline)) {
      constexpr int hf = decltype(hc)::value;
      PACE();
      u32x2 sm;
      if (hf == 0) sm = __builtin_amdgcn_raw_buffer_load_b64(rs, lane * 8, smoff, AUX_SC1);
      u32x4 nb[4];
#pragma unroll
      for (int n = 0; n < 4; ++n)
        nb[n] = hf == 0 ? __builtin_amdgcn_raw_buffer_load_b128(rk, voffk + n * 1024, base + 32768, AUX_NT)
                        : __builtin_amdgcn_raw_buffer_load_b128(rn, voffn + n * 1024, nbase, AUX_NT);
      __builtin_amdgcn_sched_barrier(0);
      static_for<0, 4>([&](auto uc) __attribute__((always_inline)) {
        constexpr int u = decltype(uc)::value + 4 * hf, bank = u >> 1, reg = u & 1;
        const f32x4 d = mfma16(buf[u & 3], qf, f32x4{0.f, 0.f, 0.f, 0.f});
        if constexpr (bank == 0) x[reg] = score_sum<0>(d);
        else x[reg] = dpp_bank<bank>(x[reg], score_sum<bank>(d));
      });
      asm volatile("" : "+v"(x));
#pragma unroll
      for (int n = 0; n < 4; ++n) buf[n] = nb[n];
      __builtin_amdgcn_sched_barrier(0);
      if (hf == 0 && it == 3) tag = __builtin_amdgcn_readfirstlane(sm[0]) + 1u;
      if (hf == 0 && it == 1) {
        const unsigned long long dd = (((unsigned long long)sm[1] << 32) | sm[0]) - t0 + 1024;
        float d = dd < 2048 ? (float)((int)dd - 1024) : (float)(PER16 / 8);
        d += dpp<0xB1>(d); d += dpp<0x4E>(d); d += dpp<ROR4>(d); d += dpp<ROR8>(d); d = xrow_sum(d);
        delta = first_tick(__builtin_amdgcn_readfirstlane((int)d) >> 2) + 3 * PER16 - tnext;
      }
    });
    if (it == gw) tnext += delta;
    *reinterpret_cast<f32x2*>(wl + it * 128 + 2 * lane) = x;
    mx = fmaxf(mx, fmaxf(x[0], x[1]));
  }
  asm volatile("" :: "v"(vtouch), "v"(ctouch));

  mx = fmaxf(mx, dpp<ROR4>(mx)); mx = fmaxf(mx, dpp<ROR8>(mx)); mx = xrow_max(mx);
  const float m_w = mx * C2;
  {
    float l_w = 0.f;
    unsigned* pl = reinterpret_cast<unsigned*>(wl + 1024) + (lane & 0x30) + (lane & 3) * 4 + ((lane >> 2) & 3);
#pragma unroll
    for (int it = 0; it < NIT; ++it) {
      const f32x2 x = *reinterpret_cast<const f32x2*>(wl + it * 128 + 2 * lane);
      const float p0 = __builtin_amdgcn_exp2f(fmaf(x[0], C2, -m_w)), p1 = __builtin_amdgcn_exp2f(fmaf(x[1], C2, -m_w));
      l_w += p0 + p1;
      pl[it * 64] = pack_bf16(p0, p1);
    }
    l_w += dpp<ROR4>(l_w); l_w += dpp<ROR8>(l_w); l_w = xrow_sum(l_w);
    if (lane < 4) *reinterpret_cast<f32x2*>(lds + L_ML + wave * 8 + 2 * lane) = f32x2{m_w, l_w};
    if (wave == 0) {
      const float t = score_sum<0>(mfma16(ktf, qf, f32x4{0.f, 0.f, 0.f, 0.f}));
      const float mask = __uint_as_float((bh & 8) ? tmw & 0xffff0000u : tmw << 16);
      const float st = (t * SCALE + (1.0f - mask) * -1000000000.0f) * LOG2E;
      if (lane < 4) *reinterpret_cast<f32x2*>(lds + L_ML + NW * 8 + 2 * lane) = s == 0 ? f32x2{st, 1.0f} : f32x2{m_w, 0.0f};
    }
    unsigned arrived = 0;
    if (lane == 0) arrived = __hip_atomic_fetch_add(reinterpret_cast<unsigned*>(lds) + L_CNT, 1u, __ATOMIC_ACQ_REL, __HIP_MEMORY_SCOPE_WORKGROUP);
    if (__builtin_amdgcn_readfirstlane(arrived) == NW - 1) {
      const f32x2 ml = *reinterpret_cast<const f32x2*>(lds + L_ML + 2 * (lane < 36 ? lane : lane & 3));
      float M = ml[0];
      M = fmaxf(M, dpp<ROR4>(M)); M = fmaxf(M, dpp<ROR8>(M)); M = xrow_max(M);
      float L = lane < 36 ? ml[1] * __builtin_amdgcn_exp2f(ml[0] - M) : 0.f;
      L += dpp<ROR4>(L); L += dpp<ROR8>(L); L = xrow_sum(L);
      if (lane < 4) __builtin_amdgcn_raw_buffer_store_b128(u32x4{__float_as_uint(M), tag, __float_as_uint(L), tag}, rw, 512 + lane * 16, s * 64, AUX_SC1);
    }
  }

  f32x4 acc[8];
#pragma unroll
  for (int j = 0; j < 8; ++j) acc[j] = f32x4{0.f, 0.f, 0.f, 0.f};
  float fw = 0.f, ft = 0.f;
  const unsigned* pa = reinterpret_cast<const unsigned*>(wl + 1024) + (lane >> 2) * 4;
#pragma clang loop unroll(disable)
  for (int it = 0; it < NIT; ++it) {
    const unsigned base = it * 65536;
    const u32x4 a = *reinterpret_cast<const u32x4*>(pa + it * 64);
    u32x4 bv[8], gs;
    __builtin_amdgcn_sched_barrier(0);
    static_for<0, 2>([&](auto hc) __attribute__((always_inline)) {
      constexpr int hf = decltype(hc)::value;
      if (hf == 0 || it < NIT - 1) PACE();
      if (hf == 1) gs = __builtin_amdgcn_raw_buffer_load_b128(rw, 512 + (lane & 15) * 16, it == NIT - 4 ? 0 : HSTR, AUX_SC1);
      u32x4 nb[4];
#pragma unroll
      for (int n = 0; n < 4; ++n) nb[n] = __builtin_amdgcn_raw_buffer_load_b128(rv, voffv + n * 1024, base + 32768 * (hf + 1), AUX_NT);
      __builtin_amdgcn_sched_barrier(0);
#pragma unroll
      for (int w = 0; w < 2; ++w)
#pragma unroll
        for (int j = 0; j < 8; ++j) {
          unsigned t = __builtin_amdgcn_perm(buf[2 * w + 1][j >> 1], buf[2 * w][j >> 1], (j & 1) ? 0x07060302u : 0x05040100u);
          if (hf == 0) asm volatile("" : "+v"(t));
          bv[j][2 * hf + w] = t;
        }
#pragma unroll
      for (int n = 0; n < 4; ++n) buf[n] = nb[n];
      __builtin_amdgcn_sched_barrier(0);
    });
#pragma unroll
    for (int j = 0; j < 8; ++j) acc[j] = mfma16(a, bv[j], acc[j]);
    if (it + 8 == gw) tnext += delta;
    if (it == NIT - 4) {
      if (!__all(gs[1] == tag && gs[3] == tag)) {
        unsigned spins = 0;
        do {
          __builtin_amdgcn_s_sleep(8);
          asm volatile("" ::: "memory");
          gs = __builtin_amdgcn_raw_buffer_load_b128(rw, 512 + (lane & 15) * 16, 0, AUX_SC1);
        } while (!__all(gs[1] == tag && gs[3] == tag) && ++spins < SPIN_CAP);
      }
      __builtin_amdgcn_raw_buffer_store_b32(tag, rw, (tid | s) * HSTR, 0, AUX_SC1);
      float M = __uint_as_float(gs[0]);
      M = fmaxf(M, dpp<ROR4>(M)); M = fmaxf(M, dpp<ROR8>(M));
      float L = __uint_as_float(gs[2]) * __builtin_amdgcn_exp2f(__uint_as_float(gs[0]) - M);
      L += dpp<ROR4>(L); L += dpp<ROR8>(L);
      const float inv = 1.0f / L;
      fw = __shfl(__builtin_amdgcn_exp2f(m_w - M) * inv, lane >> 4);
      asm volatile("" ::: "memory");
      const f32x2 tr = *reinterpret_cast<const f32x2*>(lds + L_ML + NW * 8 + 2 * (lane & 3));
      ft = __builtin_bit_cast(float, __builtin_amdgcn_readlane(__builtin_bit_cast(int, tr[1] * __builtin_amdgcn_exp2f(tr[0] - M) * inv), wave & 3));
    }
  }

  *reinterpret_cast<f32x4*>(wl + lane * 8) = f32x4{acc[0][0] * fw, acc[1][0] * fw, acc[2][0] * fw, acc[3][0] * fw};
  *reinterpret_cast<f32x4*>(wl + lane * 8 + 4) = f32x4{acc[4][0] * fw, acc[5][0] * fw, acc[6][0] * fw, acc[7][0] * fw};
  __syncthreads();
  if (wave < G) {
    f32x2 ov[NW];
#pragma unroll
    for (int w = 0; w < NW; ++w) ov[w] = *reinterpret_cast<const f32x2*>(lds + w * WLDS + wave * D + 2 * lane);
    const f32x2 ot = {ft * __uint_as_float(vtu << 16), ft * __uint_as_float(vtu & 0xffff0000u)};
    f32x2 o = (((ot + ov[0]) + ov[1]) + (ov[2] + ov[3])) + ((ov[4] + ov[5]) + (ov[6] + ov[7]));
    if (wave != (int)s) {
      __builtin_amdgcn_raw_buffer_store_b128(u32x4{__float_as_uint(o[0]), tag, __float_as_uint(o[1]), tag}, rw, lane * 16, 1024 + (wave * NS + s) * 1024, AUX_SC1);
    } else {
      u32x4 g[NS - 1];
      __builtin_amdgcn_s_sleep(12);
      for (unsigned spins = 0;; ++spins) {
        bool ok = true;
        asm volatile("" ::: "memory");
#pragma unroll
        for (int k = 0; k < NS - 1; ++k) g[k] = __builtin_amdgcn_raw_buffer_load_b128(rw, lane * 16, 1024 + (s * NS + ((s + 1 + k) & 3)) * 1024, AUX_SC1);
#pragma unroll
        for (int k = 0; k < NS - 1; ++k) ok &= g[k][1] == tag && g[k][3] == tag;
        if (__all(ok) || spins >= SPIN_CAP) break;
      }
#pragma unroll
      for (int k = 0; k < NS - 1; ++k) { o[0] += __uint_as_float(g[k][0]); o[1] += __uint_as_float(g[k][2]); }
      const float pv = __uint_as_float(pack_bf16(o[0], o[1]));
      const u32x4 w4 = {__float_as_uint(pv), __float_as_uint(dpp<0x55>(pv)), __float_as_uint(dpp<0xAA>(pv)), __float_as_uint(dpp<0xFF>(pv))};
      __builtin_amdgcn_raw_buffer_store_b128(w4, make_rsrc((const void*)ka[6], B_ * HQ * D * 2), (lane & 3) ? 0x40000000 : lane * 4, bid * (D * 2), AUX_SC1);
    }
  }
  asm volatile(".Lattn_code_tail:");
}
}

extern "C" void kernel_launch(void* const* d_in, const int* in_sizes, int n_in, void* d_out, int out_size, void* d_ws, size_t ws_size,
                              hipStream_t stream) {
  attn_kernel<<<NWG, NT, 0, stream>>>(d_in[0], d_in[1], d_in[2], d_in[3], d_in[4], d_in[5], d_out, d_ws);
}
```

```cpp
#include <hip/hip_runtime.h>
#include <hip/hip_bf16.h>

#define P_PACE 3450
#define P_OFF 80
#define P_PAUSE 30
#define P_POLL 106

namespace {
constexpr int B_ = 8, HQ = 32, HKV = 8, CTX = 8192, D = 128, G = HQ / HKV;
constexpr int NW = 8, NS = 4, NWG = B_ * HKV * NS, NT = NW * 64;
constexpr int NIT = CTX / 32 / (NS * NW);
constexpr float SCALE = 0.088388347648318440f, LOG2E = 1.4426950408889634f, C2 = SCALE * LOG2E;
static_assert(NIT == 8 && NS == G, "layout");

typedef unsigned u32x4 __attribute__((ext_vector_type(4)));
typedef unsigned u32x2 __attribute__((ext_vector_type(2)));
typedef unsigned long long u64x8 __attribute__((ext_vector_type(8)));
typedef float f32x4 __attribute__((ext_vector_type(4)));
typedef float f32x2 __attribute__((ext_vector_type(2)));
typedef __bf16 bf16x8_t __attribute__((ext_vector_type(8)));
typedef __bf16 bf16x2_t __attribute__((ext_vector_type(2)));
#define GAS __attribute__((address_space(1)))
constexpr int AUX_NT = 2, AUX_SC1 = 16;

__device__ __forceinline__ f32x4 mfma16(u32x4 a, u32x4 b, f32x4 c) {
  return __builtin_amdgcn_mfma_f32_16x16x32_bf16(__builtin_bit_cast(bf16x8_t, a), __builtin_bit_cast(bf16x8_t, b), c, 0, 0, 0);
}
template <int CTRL> __device__ __forceinline__ float dpp(float x) {
  return __builtin_bit_cast(float, __builtin_amdgcn_mov_dpp(__builtin_bit_cast(int, x), CTRL, 0xf, 0xf, true));
}
template <int BANK> __device__ __forceinline__ float dpp_bank(float old, float x) {
  return __builtin_bit_cast(float, __builtin_amdgcn_update_dpp(__builtin_bit_cast(int, old), __builtin_bit_cast(int, x), 0xE4, 0xf, 1 << BANK, false));
}
constexpr int ROR4 = 0x124, ROR8 = 0x128, ROR12 = 0x12C;
__device__ __forceinline__ float xrow_max(float x) {
  auto s = __builtin_amdgcn_permlane16_swap(__float_as_uint(x), __float_as_uint(x), false, false);
  x = fmaxf(__uint_as_float(s[0]), __uint_as_float(s[1]));
  auto t = __builtin_amdgcn_permlane32_swap(__float_as_uint(x), __float_as_uint(x), false, false);
  return fmaxf(__uint_as_float(t[0]), __uint_as_float(t[1]));
}
__device__ __forceinline__ float xrow_sum(float x) {
  auto s = __builtin_amdgcn_permlane16_swap(__float_as_uint(x), __float_as_uint(x), false, false);
  x = __uint_as_float(s[0]) + __uint_as_float(s[1]);
  auto t = __builtin_amdgcn_permlane32_swap(__float_as_uint(x), __float_as_uint(x), false, false);
  return __uint_as_float(t[0]) + __uint_as_float(t[1]);
}
__device__ __forceinline__ unsigned pack_bf16(float lo, float hi) {
  bf16x2_t v = {(__bf16)lo, (__bf16)hi};
  return __builtin_bit_cast(unsigned, v);
}
template <int I> __device__ __forceinline__ float score_sum(f32x4 d) {
  float t = d[I];
  t += dpp<ROR4>(d[(I + 3) & 3]);
  t += dpp<ROR8>(d[(I + 2) & 3]);
  t += dpp<ROR12>(d[(I + 1) & 3]);
  return t;
}
template <int I> struct IC { static constexpr int value = I; };
template <int I, int E, typename F> __device__ __forceinline__ void static_for(F&& f) {
  if constexpr (I < E) { f(IC<I>{}); static_for<I + 1, E>(f); }
}
__device__ __forceinline__ auto make_rsrc(const void* p, unsigned bytes) { return __builtin_amdgcn_make_buffer_rsrc((void*)p, 0, bytes, 0x00020000); }

constexpr unsigned WS_HEADS = 1024, HSTR = 1024 + NS * NS * 1024, WS_END = WS_HEADS + B_ * HKV * HSTR;
constexpr int WLDS = 1536, L_ML = NW * WLDS, L_CNT = L_ML + (NW + 1) * 8, LDS_N = L_CNT + 4;
constexpr unsigned SPIN_CAP = 1u << 18;
constexpr unsigned PER16 = P_PACE / 2;
constexpr unsigned WG_BYTES = CTX * D * 2 / NS;

__global__ void __launch_bounds__(NT, 2) attn_kernel(const void* q_, const void* kc_, const void* vc_, const void* kt_, const void* vt_,
                                                     const void* tm_, void* out_, void* ws_) {
  __shared__ __attribute__((aligned(16))) float lds[LDS_N];
  u64x8 ka;
  unsigned long long pc; unsigned toff;
  const int tid = threadIdx.x;
  const unsigned bid = blockIdx.x;
  asm volatile("s_load_dwordx16 %0, %1, 0x0" : "=s"(ka) : "s"(__builtin_amdgcn_kernarg_segment_ptr()));
  __builtin_amdgcn_sched_barrier(0);
  unsigned tx = (tid << 6) ^ (bid << 9);
  asm volatile("s_getpc_b64 %0\n.Lattn_code_here:\n\tv_min_u32 %1, .Lattn_code_tail-.Lattn_code_here-4, %2" : "=s"(pc), "=&v"(toff), "+v"(tx));
  const unsigned ctouch = *(const GAS unsigned*)((const GAS char*)pc + toff);
  int voffk = (tx & 0x7300) | ((tid << 4) & 0x30) | ((tid << 2) & 0xc0);
  unsigned koff = bid * WG_BYTES + voffk;
  asm volatile("" : "+v"(koff));
  __builtin_amdgcn_sched_barrier(0);
  asm volatile("s_waitcnt lgkmcnt(0)" : "+s"(ka));
  u32x4 buf[4];
#pragma unroll
  for (int n = 0; n < 4; ++n) buf[n] = __builtin_nontemporal_load((const GAS u32x4*)((const GAS char*)ka[1] + koff + n * 1024));
  __builtin_amdgcn_sched_barrier(0);
  const unsigned long long t0 = __builtin_amdgcn_s_memrealtime();
  __builtin_amdgcn_sched_barrier(0);
  const int lane = tid & 63;
  const int wave = __builtin_amdgcn_readfirstlane(tid >> 6);
  const unsigned s = bid & 3, bh = bid >> 2;
  const u32x4 qf = *(const GAS u32x4*)((const GAS char*)ka[0] + (unsigned)(((bh * G + (lane & 3)) * D + (lane >> 2) * 8) * 2));
  __builtin_amdgcn_sched_barrier(0);
  const char* kb = (const char*)ka[1] + (size_t)bid * WG_BYTES;
  const char* vb = (const char*)ka[2] + (size_t)bid * WG_BYTES;
  unsigned char* ws = (unsigned char*)ka[7];
  const auto rk = make_rsrc(kb, WG_BYTES), rv = make_rsrc(vb, WG_BYTES);
  const int voffv = ((tx & 0x7000) | (lane * 16)) ^ ((bid & 1) << 9);
  unsigned char* wsh = ws + WS_HEADS + bh * HSTR;
  const auto rw = make_rsrc(wsh, HSTR);
  const unsigned t016 = ((unsigned)t0 & 0x7ffffffu) * 16u;
  const unsigned rot = (__builtin_popcount(bid & 0x63) & 1) | ((__builtin_popcount(bid & 0xb6) & 1) << 1) | ((__builtin_popcount(bid & 0xca) & 1) << 2);
  const unsigned ph = (wave ^ rot ^ ((bid >> 3) & 7)) * (PER16 / NW) + (((bid & 3) * 2) | ((bid >> 3) & 24)) * (PER16 / 256);
  auto first_tick = [&](int origin) -> unsigned { const unsigned t = t016 + origin + (P_OFF * 16 + PER16 - 1); return t - (t + 64 * PER16 - ph) % PER16; };
  unsigned tnext = first_tick(0), delta = 0, tpoll = t016;
  const auto rs = make_rsrc(ws, WS_END);
  if ((unsigned)wave == (bid >> 5) && (bid & 24) == 0 && lane == 0)
    __builtin_amdgcn_raw_buffer_store_b64(u32x2{(unsigned)t0, (unsigned)(t0 >> 32)}, rs, 0, (bid >> 5) * 64 + (bid & 7) * 8, AUX_SC1);
  int gw = 2 + (int)(((bid >> 3) * 5 + wave * 3) % 10);
  gw += gw >= 7; gw += gw >= 12;
#define BEFORE(t) ((int)(((((unsigned)__builtin_amdgcn_s_memrealtime() & 0x7ffffffu) * 16u) - (t)) << 1) < 0)
#define PACE() do { while (BEFORE(tnext)) __builtin_amdgcn_s_sleep(1); tnext += PER16; __builtin_amdgcn_sched_barrier(0); } while (0)
  if (tid == 0) reinterpret_cast<unsigned*>(lds)[L_CNT] = 0u;
  __syncthreads();
  unsigned tag = 0;
  const u32x4 ktf = __builtin_amdgcn_raw_buffer_load_b128(make_rsrc((const char*)ka[3] + bh * (D * 2), (s | wave) == 0 ? D * 2 : 0), voffk & 0xf0, 0, 0);
  const unsigned vtu = __builtin_amdgcn_raw_buffer_load_b32(make_rsrc((const char*)ka[4] + bh * (D * 2), (s | (wave >> 2)) == 0 ? D * 2 : 0), lane * 4, 0, 0);
  const unsigned tmw = __builtin_amdgcn_raw_buffer_load_b32(make_rsrc((const void*)ka[5], (s | wave) == 0 ? B_ * 2 : 0), 0, (bh >> 4) * 4, 0);
  const unsigned vtouch = __builtin_amdgcn_raw_buffer_load_b32(rv, 0, (unsigned)(wave ^ 1) * WG_BYTES, AUX_NT);
  float* wl = lds + wave * WLDS;
  float mx = -INFINITY;
  __builtin_amdgcn_sched_barrier(0);

#pragma clang loop unroll(disable)
  for (int it = 0; it < NIT; ++it) {
    const bool last = it == NIT - 1;
    const unsigned base = it * 65536, nbase = last ? 0 : base + 65536;
    const auto rn = make_rsrc(last ? vb : kb, WG_BYTES);
    const int voffn = last ? voffv : voffk;
    unsigned smoff = it == 4 ? WS_HEADS + bh * HSTR : WS_END;
    smoff = it == 2 ? 0 : smoff;
    asm volatile("" : "+s"(smoff));
    f32x2 x;
    static_for<0, 2>([&](auto hc) __attribute__((always_inline)) {
      constexpr int hf = decltype(hc)::value;
      PACE();
      u32x2 sm;
      if (hf == 0) sm = __builtin_amdgcn_raw_buffer_load_b64(rs, lane * 8, smoff, AUX_SC1);
      u32x4 nb[4];
#pragma unroll
      for (int n = 0; n < 4; ++n)
        nb[n] = hf == 0 ? __builtin_amdgcn_raw_buffer_load_b128(rk, voffk + n * 1024, base + 32768, AUX_NT)
                        : __builtin_amdgcn_raw_buffer_load_b128(rn, voffn + n * 1024, nbase, AUX_NT);
      __builtin_amdgcn_sched_barrier(0);
      static_for<0, 4>([&](auto uc) __attribute__((always_inline)) {
        constexpr int u = decltype(uc)::value + 4 * hf, bank = u >> 1, reg = u & 1;
        const f32x4 d = mfma16(buf[u & 3], qf, f32x4{0.f, 0.f, 0.f, 0.f});
        if constexpr (bank == 0) x[reg] = score_sum<0>(d);
        else x[reg] = dpp_bank<bank>(x[reg], score_sum<bank>(d));
      });
      asm volatile("" : "+v"(x));
#pragma unroll
      for (int n = 0; n < 4; ++n) buf[n] = nb[n];
      __builtin_amdgcn_sched_barrier(0);
      if (hf == 0 && last) tnext += P_PAUSE * 16;
      if (hf == 0 && it == 4) tag = __builtin_amdgcn_readfirstlane(sm[0]) + 1u;
      if (hf == 0 && it == 2) {
        const unsigned long long dd = (((unsigned long long)sm[1] << 32) | sm[0]) - t0 + 1024;
        float d = dd < 2048 ? (float)((int)dd - 1024) : (float)(PER16 / 8);
        d += dpp<0xB1>(d); d += dpp<0x4E>(d); d += dpp<ROR4>(d); d += dpp<ROR8>(d); d = xrow_sum(d);
        const int origin = __builtin_amdgcn_readfirstlane((int)d) >> 2;
        tpoll = t016 + origin + (P_OFF + P_PAUSE + P_POLL) * 16 + 31 * PER16;
        delta = first_tick(origin) + 5 * PER16 - tnext;
      }
    });
    if (it == gw) tnext += delta;
    *reinterpret_cast<f32x2*>(wl + it * 128 + 2 * lane) = x;
    mx = fmaxf(mx, fmaxf(x[0], x[1]));
  }
  asm volatile("" :: "v"(vtouch), "v"(ctouch));

  mx = fmaxf(mx, dpp<ROR4>(mx)); mx = fmaxf(mx, dpp<ROR8>(mx)); mx = xrow_max(mx);
  const float m_w = mx * C2;
  {
    float l_w = 0.f;
    unsigned* pl = reinterpret_cast<unsigned*>(wl + 1024) + (lane & 0x30) + (lane & 3) * 4 + ((lane >> 2) & 3);
#pragma unroll
    for (int it = 0; it < NIT; ++it) {
      const f32x2 x = *reinterpret_cast<const f32x2*>(wl + it * 128 + 2 * lane);
      const float p0 = __builtin_amdgcn_exp2f(fmaf(x[0], C2, -m_w)), p1 = __builtin_amdgcn_exp2f(fmaf(x[1], C2, -m_w));
      l_w += p0 + p1;
      pl[it * 64] = pack_bf16(p0, p1);
    }
    l_w += dpp<ROR4>(l_w); l_w += dpp<ROR8>(l_w); l_w = xrow_sum(l_w);
    if (lane < 4) *reinterpret_cast<f32x2*>(lds + L_ML + wave * 8 + 2 * lane) = f32x2{m_w, l_w};
    if (wave == 0) {
      const float t = score_sum<0>(mfma16(ktf, qf, f32x4{0.f, 0.f, 0.f, 0.f}));
      const float mask = __uint_as_float((bh & 8) ? tmw & 0xffff0000u : tmw << 16);
      const float st = (t * SCALE + (1.0f - mask) * -1000000000.0f) * LOG2E;
      if (lane < 4) *reinterpret_cast<f32x2*>(lds + L_ML + NW * 8 + 2 * lane) = s == 0 ? f32x2{st, 1.0f} : f32x2{m_w, 0.0f};
    }
    unsigned arrived = 0;
    if (lane == 0) arrived = __hip_atomic_fetch_add(reinterpret_cast<unsigned*>(lds) + L_CNT, 1u, __ATOMIC_ACQ_REL, __HIP_MEMORY_SCOPE_WORKGROUP);
    if (__builtin_amdgcn_readfirstlane(arrived) == NW - 1) {
      const f32x2 ml = *reinterpret_cast<const f32x2*>(lds + L_ML + 2 * (lane < 36 ? lane : lane & 3));
      float M = ml[0];
      M = fmaxf(M, dpp<ROR4>(M)); M = fmaxf(M, dpp<ROR8>(M)); M = xrow_max(M);
      float L = lane < 36 ? ml[1] * __builtin_amdgcn_exp2f(ml[0] - M) : 0.f;
      L += dpp<ROR4>(L); L += dpp<ROR8>(L); L = xrow_sum(L);
      if (lane < 4) __builtin_amdgcn_raw_buffer_store_b128(u32x4{__float_as_uint(M), tag, __float_as_uint(L), tag}, rw, 512 + lane * 16, s * 64, AUX_SC1);
    }
  }

  f32x4 acc[8];
#pragma unroll
  for (int j = 0; j < 8; ++j) acc[j] = f32x4{0.f, 0.f, 0.f, 0.f};
  float fw = 0.f, ft = 0.f;
  const unsigned* pa = reinterpret_cast<const unsigned*>(wl + 1024) + (lane >> 2) * 4;
#pragma clang loop unroll(disable)
  for (int it = 0; it < NIT; ++it) {
    const unsigned base = it * 65536;
    const u32x4 a = *reinterpret_cast<const u32x4*>(pa + it * 64);
    u32x4 bv[8], gs;
    __builtin_amdgcn_sched_barrier(0);
    static_for<0, 2>([&](auto hc) __attribute__((always_inline)) {
      constexpr int hf = decltype(hc)::value;
      if (hf == 0 || it < NIT - 1) PACE();
      if (hf == 1) gs = __builtin_amdgcn_raw_buffer_load_b128(rw, 512 + (lane & 15) * 16, it == NIT - 4 ? 0 : HSTR, AUX_SC1);
      u32x4 nb[4];
#pragma unroll
      for (int n = 0; n < 4; ++n) nb[n] = __builtin_amdgcn_raw_buffer_load_b128(rv, voffv + n * 1024, base + 32768 * (hf + 1), AUX_NT);
      __builtin_amdgcn_sched_barrier(0);
#pragma unroll
      for (int w = 0; w < 2; ++w)
#pragma unroll
        for (int j = 0; j < 8; ++j) {
          unsigned t = __builtin_amdgcn_perm(buf[2 * w + 1][j >> 1], buf[2 * w][j >> 1], (j & 1) ? 0x07060302u : 0x05040100u);
          if (hf == 0) asm volatile("" : "+v"(t));
          bv[j][2 * hf + w] = t;
        }
#pragma unroll
      for (int n = 0; n < 4; ++n) buf[n] = nb[n];
      __builtin_amdgcn_sched_barrier(0);
    });
#pragma unroll
    for (int j = 0; j < 8; ++j) acc[j] = mfma16(a, bv[j], acc[j]);
    if (it + 8 == gw) tnext += delta;
    if (it == NIT - 4) {
      if (!__all(gs[1] == tag && gs[3] == tag)) {
        unsigned spins = 0;
        do {
          __builtin_amdgcn_s_sleep(8);
          asm volatile("" ::: "memory");
          gs = __builtin_amdgcn_raw_buffer_load_b128(rw, 512 + (lane & 15) * 16, 0, AUX_SC1);
        } while (!__all(gs[1] == tag && gs[3] == tag) && ++spins < SPIN_CAP);
      }
      __builtin_amdgcn_raw_buffer_store_b32(tag, rw, (tid | s) * HSTR, 0, AUX_SC1);
      float M = __uint_as_float(gs[0]);
      M = fmaxf(M, dpp<ROR4>(M)); M = fmaxf(M, dpp<ROR8>(M));
      float L = __uint_as_float(gs[2]) * __builtin_amdgcn_exp2f(__uint_as_float(gs[0]) - M);
      L += dpp<ROR4>(L); L += dpp<ROR8>(L);
      const float inv = 1.0f / L;
      fw = __shfl(__builtin_amdgcn_exp2f(m_w - M) * inv, lane >> 4);
      asm volatile("" ::: "memory");
      const f32x2 tr = *reinterpret_cast<const f32x2*>(lds + L_ML + NW * 8 + 2 * (lane & 3));
      ft = __builtin_bit_cast(float, __builtin_amdgcn_readlane(__builtin_bit_cast(int, tr[1] * __builtin_amdgcn_exp2f(tr[0] - M) * inv), wave & 3));
    }
  }

  *reinterpret_cast<f32x4*>(wl + lane * 8) = f32x4{acc[0][0] * fw, acc[1][0] * fw, acc[2][0] * fw, acc[3][0] * fw};
  *reinterpret_cast<f32x4*>(wl + lane * 8 + 4) = f32x4{acc[4][0] * fw, acc[5][0] * fw, acc[6][0] * fw, acc[7][0] * fw};
  __syncthreads();
  if (wave < G) {
    f32x2 ov[NW];
#pragma unroll
    for (int w = 0; w < NW; ++w) ov[w] = *reinterpret_cast<const f32x2*>(lds + w * WLDS + wave * D + 2 * lane);
    const f32x2 ot = {ft * __uint_as_float(vtu << 16), ft * __uint_as_float(vtu & 0xffff0000u)};
    f32x2 o = (((ot + ov[0]) + ov[1]) + (ov[2] + ov[3])) + ((ov[4] + ov[5]) + (ov[6] + ov[7]));
    if (wave != (int)s) {
      __builtin_amdgcn_raw_buffer_store_b128(u32x4{__float_as_uint(o[0]), tag, __float_as_uint(o[1]), tag}, rw, lane * 16, 1024 + (wave * NS + s) * 1024, AUX_SC1);
    } else {
      u32x4 g[NS - 1];
      __builtin_amdgcn_s_sleep(6);
      while (BEFORE(tpoll)) {}
      for (unsigned spins = 0;; ++spins) {
        bool ok = true;
        asm volatile("" ::: "memory");
#pragma unroll
        for (int k = 0; k < NS - 1; ++k) g[k] = __builtin_amdgcn_raw_buffer_load_b128(rw, lane * 16, 1024 + (s * NS + ((s + 1 + k) & 3)) * 1024, AUX_SC1);
#pragma unroll
        for (int k = 0; k < NS - 1; ++k) ok &= g[k][1] == tag && g[k][3] == tag;
        if (__all(ok) || spins >= SPIN_CAP) break;
      }
#pragma unroll
      for (int k = 0; k < NS - 1; ++k) { o[0] += __uint_as_float(g[k][0]); o[1] += __uint_as_float(g[k][2]); }
      const float pv = __uint_as_float(pack_bf16(o[0], o[1]));
      const u32x4 w4 = {__float_as_uint(pv), __float_as_uint(dpp<0x55>(pv)), __float_as_uint(dpp<0xAA>(pv)), __float_as_uint(dpp<0xFF>(pv))};
      __builtin_amdgcn_raw_buffer_store_b128(w4, make_rsrc((const void*)ka[6], B_ * HQ * D * 2), (lane & 3) ? 0x40000000 : lane * 4, bid * (D * 2), AUX_SC1);
    }
  }
  asm volatile(".Lattn_code_tail:");
}
}

extern "C" void kernel_launch(void* const* d_in, const int* in_sizes, int n_in, void* d_out, int out_size, void* d_ws, size_t ws_size,
                              hipStream_t stream) {
  attn_kernel<<<NWG, NT, 0, stream>>>(d_in[0], d_in[1], d_in[2], d_in[3], d_in[4], d_in[5], d_out, d_ws);
}
```

```cpp
#include <hip/hip_runtime.h>
#include <hip/hip_bf16.h>

#define P_PACE 3440
#define P_OFF 92
#define P_PAUSE 10
#define P_POLL 106

namespace {
constexpr int B_ = 8, HQ = 32, HKV = 8, CTX = 8192, D = 128, G = HQ / HKV;
constexpr int NW = 8, NS = 4, NWG = B_ * HKV * NS, NT = NW * 64;
constexpr int NIT = CTX / 32 / (NS * NW);
constexpr float SCALE = 0.088388347648318440f, LOG2E = 1.4426950408889634f, C2 = SCALE * LOG2E;
static_assert(NIT == 8 && NS == G, "layout");

typedef unsigned u32x4 __attribute__((ext_vector_type(4)));
typedef unsigned u32x2 __attribute__((ext_vector_type(2)));
typedef unsigned long long u64x8 __attribute__((ext_vector_type(8)));
typedef float f32x4 __attribute__((ext_vector_type(4)));
typedef float f32x2 __attribute__((ext_vector_type(2)));
typedef __bf16 bf16x8_t __attribute__((ext_vector_type(8)));
typedef __bf16 bf16x2_t __attribute__((ext_vector_type(2)));
#define GAS __attribute__((address_space(1)))
constexpr int AUX_NT = 2, AUX_SC1 = 16;

__device__ __forceinline__ f32x4 mfma16(u32x4 a, u32x4 b, f32x4 c) {
  return __builtin_amdgcn_mfma_f32_16x16x32_bf16(__builtin_bit_cast(bf16x8_t, a), __builtin_bit_cast(bf16x8_t, b), c, 0, 0, 0);
}
template <int CTRL> __device__ __forceinline__ float dpp(float x) {
  return __builtin_bit_cast(float, __builtin_amdgcn_mov_dpp(__builtin_bit_cast(int, x), CTRL, 0xf, 0xf, true));
}
template <int BANK> __device__ __forceinline__ float dpp_bank(float old, float x) {
  return __builtin_bit_cast(float, __builtin_amdgcn_update_dpp(__builtin_bit_cast(int, old), __builtin_bit_cast(int, x), 0xE4, 0xf, 1 << BANK, false));
}
constexpr int ROR4 = 0x124, ROR8 = 0x128, ROR12 = 0x12C;
__device__ __forceinline__ float xrow_max(float x) {
  auto s = __builtin_amdgcn_permlane16_swap(__float_as_uint(x), __float_as_uint(x), false, false);
  x = fmaxf(__uint_as_float(s[0]), __uint_as_float(s[1]));
  auto t = __builtin_amdgcn_permlane32_swap(__float_as_uint(x), __float_as_uint(x), false, false);
  return fmaxf(__uint_as_float(t[0]), __uint_as_float(t[1]));
}
__device__ __forceinline__ float xrow_sum(float x) {
  auto s = __builtin_amdgcn_permlane16_swap(__float_as_uint(x), __float_as_uint(x), false, false);
  x = __uint_as_float(s[0]) + __uint_as_float(s[1]);
  auto t = __builtin_amdgcn_permlane32_swap(__float_as_uint(x), __float_as_uint(x), false, false);
  return __uint_as_float(t[0]) + __uint_as_float(t[1]);
}
__device__ __forceinline__ unsigned pack_bf16(float lo, float hi) {
  bf16x2_t v = {(__bf16)lo, (__bf16)hi};
  return __builtin_bit_cast(unsigned, v);
}
template <int I> __device__ __forceinline__ float score_sum(f32x4 d) {
  float t = d[I];
  t += dpp<ROR4>(d[(I + 3) & 3]);
  t += dpp<ROR8>(d[(I + 2) & 3]);
  t += dpp<ROR12>(d[(I + 1) & 3]);
  return t;
}
template <int I> struct IC { static constexpr int value = I; };
template <int I, int E, typename F> __device__ __forceinline__ void static_for(F&& f) {
  if constexpr (I < E) { f(IC<I>{}); static_for<I + 1, E>(f); }
}
__device__ __forceinline__ auto make_rsrc(const void* p, unsigned bytes) { return __builtin_amdgcn_make_buffer_rsrc((void*)p, 0, bytes, 0x00020000); }

constexpr unsigned WS_HEADS = 1024, HSTR = 1024 + NS * NS * 1024;
constexpr int WLDS = 1536, L_ML = NW * WLDS, L_CNT = L_ML + (NW + 1) * 8, LDS_N = L_CNT + 4;
constexpr unsigned SPIN_CAP = 1u << 18;
constexpr unsigned PER16 = P_PACE / 2;
constexpr unsigned WG_BYTES = CTX * D * 2 / NS;

__global__ void __launch_bounds__(NT, 2) attn_kernel(const void* q_, const void* kc_, const void* vc_, const void* kt_, const void* vt_,
                                                     const void* tm_, void* out_, void* ws_) {
  __shared__ __attribute__((aligned(16))) float lds[LDS_N];
  u64x8 ka;
  unsigned long long pc; unsigned toff;
  const int tid = threadIdx.x;
  const unsigned bid = blockIdx.x;
  asm volatile("s_load_dwordx16 %0, %1, 0x0" : "=s"(ka) : "s"(__builtin_amdgcn_kernarg_segment_ptr()));
  __builtin_amdgcn_sched_barrier(0);
  unsigned tx = (tid << 6) ^ (bid << 9);
  asm volatile("s_getpc_b64 %0\n.Lattn_code_here:\n\tv_min_u32 %1, .Lattn_code_tail-.Lattn_code_here-4, %2" : "=s"(pc), "=&v"(toff), "+v"(tx));
  const unsigned ctouch = *(const GAS unsigned*)((const GAS char*)pc + toff);
  int voffk = (tx & 0x7300) | ((tid << 4) & 0x30) | ((tid << 2) & 0xc0);
  unsigned koff = bid * WG_BYTES + voffk;
  asm volatile("" : "+v"(koff));
  __builtin_amdgcn_sched_barrier(0);
  asm volatile("s_waitcnt lgkmcnt(0)" : "+s"(ka));
  u32x4 buf[4];
#pragma unroll
  for (int n = 0; n < 4; ++n) buf[n] = __builtin_nontemporal_load((const GAS u32x4*)((const GAS char*)ka[1] + koff + n * 1024));
  __builtin_amdgcn_sched_barrier(0);
  const unsigned long long t0 = __builtin_amdgcn_s_memrealtime();
  __builtin_amdgcn_sched_barrier(0);
  const int lane = tid & 63;
  const int wave = __builtin_amdgcn_readfirstlane(tid >> 6);
  const unsigned s = bid & 3, bh = bid >> 2;
  const u32x4 qf = *(const GAS u32x4*)((const GAS char*)ka[0] + (unsigned)(((bh * G + (lane & 3)) * D + (lane >> 2) * 8) * 2));
  __builtin_amdgcn_sched_barrier(0);
  const char* kb = (const char*)ka[1] + (size_t)bid * WG_BYTES;
  const char* vb = (const char*)ka[2] + (size_t)bid * WG_BYTES;
  unsigned char* ws = (unsigned char*)ka[7];
  const auto rk = make_rsrc(kb, WG_BYTES), rv = make_rsrc(vb, WG_BYTES);
  const int voffv = ((tx & 0x7000) | (lane * 16)) ^ ((bid & 1) << 9);
  const auto rs = make_rsrc(ws, WS_HEADS), rw = make_rsrc(ws + WS_HEADS + bh * HSTR, HSTR);
  const unsigned t016 = ((unsigned)t0 & 0x7ffffffu) * 16u;
  const unsigned rot = (__builtin_popcount(bid & 0x63) & 1) | ((__builtin_popcount(bid & 0xb6) & 1) << 1) | ((__builtin_popcount(bid & 0xca) & 1) << 2);
  const unsigned ph = (wave ^ rot ^ ((bid >> 3) & 7)) * (PER16 / NW) + (((bid & 3) * 2) | ((bid >> 3) & 24)) * (PER16 / 256);
  auto first_tick = [&](int origin) -> unsigned { const unsigned t = t016 + origin + (P_OFF * 16 + PER16 - 1); return t - (t + 64 * PER16 - ph) % PER16; };
  unsigned tnext = first_tick(0), delta = 0, tpoll = t016;
  if ((unsigned)wave == (bid >> 5) && (bid & 24) == 0 && lane == 0)
    __builtin_amdgcn_raw_buffer_store_b64(u32x2{(unsigned)t0, (unsigned)(t0 >> 32)}, rs, 0, (bid >> 5) * 128 + (bid & 7) * 16, AUX_SC1);
  int gw = 2 + (int)(((bid >> 3) * 7 + wave * 3) % 10);
  gw += gw >= 7; gw += gw >= 12;
#define BEFORE(t) ((int)(((((unsigned)__builtin_amdgcn_s_memrealtime() & 0x7ffffffu) * 16u) - (t)) << 1) < 0)
#define PACE() do { while (BEFORE(tnext)) __builtin_amdgcn_s_sleep(1); tnext += PER16; __builtin_amdgcn_sched_barrier(0); } while (0)
  if (tid == 0) reinterpret_cast<unsigned*>(lds)[L_CNT] = 0u;
  __syncthreads();
  unsigned tag = 0;
  const u32x4 ktf = __builtin_amdgcn_raw_buffer_load_b128(make_rsrc((const char*)ka[3] + bh * (D * 2), (s | wave) == 0 ? D * 2 : 0), voffk & 0xf0, 0, 0);
  const unsigned vtu = __builtin_amdgcn_raw_buffer_load_b32(make_rsrc((const char*)ka[4] + bh * (D * 2), (s | (wave >> 2)) == 0 ? D * 2 : 0), lane * 4, 0, 0);
  const unsigned tmw = __builtin_amdgcn_raw_buffer_load_b32(make_rsrc((const void*)ka[5], (s | wave) == 0 ? B_ * 2 : 0), 0, (bh >> 4) * 4, 0);
  const unsigned vtouch = __builtin_amdgcn_raw_buffer_load_b32(rv, 0, (unsigned)(wave ^ 1) * WG_BYTES, AUX_NT);
  float* wl = lds + wave * WLDS;
  float mx = -INFINITY;
  __builtin_amdgcn_sched_barrier(0);

#pragma clang loop unroll(disable)
  for (int it = 0; it < NIT; ++it) {
    const bool last = it == NIT - 1;
    const unsigned base = it * 65536, nbase = last ? 0 : base + 65536;
    const auto rn = make_rsrc(last ? vb : kb, WG_BYTES);
    const int voffn = last ? voffv : voffk;
    u32x4 sm;
    f32x2 x;
    static_for<0, 2>([&](auto hc) __attribute__((always_inline)) {
      constexpr int hf = decltype(hc)::value;
      PACE();
      if (hf == 0) sm = __builtin_amdgcn_raw_buffer_load_b128(rs, lane * 16, it == 2 ? 0 : WS_HEADS, AUX_SC1);
      u32x4 nb[4];
#pragma unroll
      for (int n = 0; n < 4; ++n)
        nb[n] = hf == 0 ? __builtin_amdgcn_raw_buffer_load_b128(rk, voffk + n * 1024, base + 32768, AUX_NT)
                        : __builtin_amdgcn_raw_buffer_load_b128(rn, voffn + n * 1024, nbase, AUX_NT);
      __builtin_amdgcn_sched_barrier(0);
      static_for<0, 4>([&](auto uc) __attribute__((always_inline)) {
        constexpr int u = decltype(uc)::value + 4 * hf, bank = u >> 1, reg = u & 1;
        const f32x4 d = mfma16(buf[u & 3], qf, f32x4{0.f, 0.f, 0.f, 0.f});
        if constexpr (bank == 0) x[reg] = score_sum<0>(d);
        else x[reg] = dpp_bank<bank>(x[reg], score_sum<bank>(d));
      });
      asm volatile("" : "+v"(x));
#pragma unroll
      for (int n = 0; n < 4; ++n) buf[n] = nb[n];
      __builtin_amdgcn_sched_barrier(0);
      if (hf == 0 && last) tnext += P_PAUSE * 16;
    });
    if (it == 2) {
      tag = __builtin_amdgcn_readlane(sm[2], bh) + 1u;
      const unsigned long long dd = (((unsigned long long)sm[1] << 32) | sm[0]) - t0 + 1024;
      float d = dd < 2048 ? (float)((int)dd - 1024) : (float)(PER16 / 8);
      d += dpp<0xB1>(d); d += dpp<0x4E>(d); d += dpp<ROR4>(d); d += dpp<ROR8>(d); d = xrow_sum(d);
      const int origin = __builtin_amdgcn_readfirstlane((int)d) >> 2;
      tpoll = t016 + origin + (P_OFF + P_PAUSE + P_POLL) * 16 + 31 * PER16;
      delta = first_tick(origin) + 6 * PER16 - tnext;
    }
    if (it == gw) tnext += delta;
    *reinterpret_cast<f32x2*>(wl + it * 128 + 2 * lane) = x;
    mx = fmaxf(mx, fmaxf(x[0], x[1]));
  }
  asm volatile("" :: "v"(vtouch), "v"(ctouch));

  mx = fmaxf(mx, dpp<ROR4>(mx)); mx = fmaxf(mx, dpp<ROR8>(mx)); mx = xrow_max(mx);
  const float m_w = mx * C2;
  {
    float l_w = 0.f;
    unsigned* pl = reinterpret_cast<unsigned*>(wl + 1024) + (lane & 0x30) + (lane & 3) * 4 + ((lane >> 2) & 3);
#pragma unroll
    for (int it = 0; it < NIT; ++it) {
      const f32x2 x = *reinterpret_cast<const f32x2*>(wl + it * 128 + 2 * lane);
      const float p0 = __builtin_amdgcn_exp2f(fmaf(x[0], C2, -m_w)), p1 = __builtin_amdgcn_exp2f(fmaf(x[1], C2, -m_w));
      l_w += p0 + p1;
      pl[it * 64] = pack_bf16(p0, p1);
    }
    l_w += dpp<ROR4>(l_w); l_w += dpp<ROR8>(l_w); l_w = xrow_sum(l_w);
    if (lane < 4) *reinterpret_cast<f32x2*>(lds + L_ML + wave * 8 + 2 * lane) = f32x2{m_w, l_w};
    if (wave == 0) {
      const float t = score_sum<0>(mfma16(ktf, qf, f32x4{0.f, 0.f, 0.f, 0.f}));
      const float mask = __uint_as_float((bh & 8) ? tmw & 0xffff0000u : tmw << 16);
      const float st = (t * SCALE + (1.0f - mask) * -1000000000.0f) * LOG2E;
      if (lane < 4) *reinterpret_cast<f32x2*>(lds + L_ML + NW * 8 + 2 * lane) = s == 0 ? f32x2{st, 1.0f} : f32x2{m_w, 0.0f};
    }
    unsigned arrived = 0;
    if (lane == 0) arrived = __hip_atomic_fetch_add(reinterpret_cast<unsigned*>(lds) + L_CNT, 1u, __ATOMIC_ACQ_REL, __HIP_MEMORY_SCOPE_WORKGROUP);
    if (__builtin_amdgcn_readfirstlane(arrived) == NW - 1) {
      const f32x2 ml = *reinterpret_cast<const f32x2*>(lds + L_ML + 2 * (lane < 36 ? lane : lane & 3));
      float M = ml[0];
      M = fmaxf(M, dpp<ROR4>(M)); M = fmaxf(M, dpp<ROR8>(M)); M = xrow_max(M);
      float L = lane < 36 ? ml[1] * __builtin_amdgcn_exp2f(ml[0] - M) : 0.f;
      L += dpp<ROR4>(L); L += dpp<ROR8>(L); L = xrow_sum(L);
      if (lane < 4) __builtin_amdgcn_raw_buffer_store_b128(u32x4{__float_as_uint(M), tag, __float_as_uint(L), tag}, rw, lane * 16, s * 64, AUX_SC1);
    }
  }

  f32x4 acc[8];
#pragma unroll
  for (int j = 0; j < 8; ++j) acc[j] = f32x4{0.f, 0.f, 0.f, 0.f};
  float fw = 0.f, ft = 0.f;
  const unsigned* pa = reinterpret_cast<const unsigned*>(wl + 1024) + (lane >> 2) * 4;
#pragma clang loop unroll(disable)
  for (int it = 0; it < NIT; ++it) {
    const unsigned base = it * 65536;
    const u32x4 a = *reinterpret_cast<const u32x4*>(pa + it * 64);
    u32x4 bv[8], gs;
    __builtin_amdgcn_sched_barrier(0);
    static_for<0, 2>([&](auto hc) __attribute__((always_inline)) {
      constexpr int hf = decltype(hc)::value;
      if (hf == 0 || it < NIT - 1) PACE();
      if (hf == 0) gs = __builtin_amdgcn_raw_buffer_load_b128(rw, (lane & 15) * 16, it == NIT - 4 ? 0 : HSTR, AUX_SC1);
      u32x4 nb[4];
#pragma unroll
      for (int n = 0; n < 4; ++n) nb[n] = __builtin_amdgcn_raw_buffer_load_b128(rv, voffv + n * 1024, base + 32768 * (hf + 1), AUX_NT);
      __builtin_amdgcn_sched_barrier(0);
#pragma unroll
      for (int w = 0; w < 2; ++w)
#pragma unroll
        for (int j = 0; j < 8; ++j) {
          unsigned t = __builtin_amdgcn_perm(buf[2 * w + 1][j >> 1], buf[2 * w][j >> 1], (j & 1) ? 0x07060302u : 0x05040100u);
          if (hf == 0) asm volatile("" : "+v"(t));
          bv[j][2 * hf + w] = t;
        }
#pragma unroll
      for (int n = 0; n < 4; ++n) buf[n] = nb[n];
      __builtin_amdgcn_sched_barrier(0);
    });
#pragma unroll
    for (int j = 0; j < 8; ++j) acc[j] = mfma16(a, bv[j], acc[j]);
    if (it + 8 == gw) tnext += delta;
    if (it == NIT - 4) {
      if (!__all(gs[1] == tag && gs[3] == tag)) {
        unsigned spins = 0;
        do {
          __builtin_amdgcn_s_sleep(8);
          asm volatile("" ::: "memory");
          gs = __builtin_amdgcn_raw_buffer_load_b128(rw, (lane & 15) * 16, 0, AUX_SC1);
        } while (!__all(gs[1] == tag && gs[3] == tag) && ++spins < SPIN_CAP);
      }
      __builtin_amdgcn_raw_buffer_store_b32(tag, rs, (tid | s) * WS_HEADS, bh * 16 + 8, AUX_SC1);
      float M = __uint_as_float(gs[0]);
      M = fmaxf(M, dpp<ROR4>(M)); M = fmaxf(M, dpp<ROR8>(M));
      float L = __uint_as_float(gs[2]) * __builtin_amdgcn_exp2f(__uint_as_float(gs[0]) - M);
      L += dpp<ROR4>(L); L += dpp<ROR8>(L);
      const float inv = 1.0f / L;
      fw = __shfl(__builtin_amdgcn_exp2f(m_w - M) * inv, lane >> 4);
      asm volatile("" ::: "memory");
      const f32x2 tr = *reinterpret_cast<const f32x2*>(lds + L_ML + NW * 8 + 2 * (lane & 3));
      ft = __builtin_bit_cast(float, __builtin_amdgcn_readlane(__builtin_bit_cast(int, tr[1] * __builtin_amdgcn_exp2f(tr[0] - M) * inv), wave & 3));
    }
  }

  *reinterpret_cast<f32x4*>(wl + lane * 8) = f32x4{acc[0][0] * fw, acc[1][0] * fw, acc[2][0] * fw, acc[3][0] * fw};
  *reinterpret_cast<f32x4*>(wl + lane * 8 + 4) = f32x4{acc[4][0] * fw, acc[5][0] * fw, acc[6][0] * fw, acc[7][0] * fw};
  __syncthreads();
  if (wave < G) {
    f32x2 ov[NW];
#pragma unroll
    for (int w = 0; w < NW; ++w) ov[w] = *reinterpret_cast<const f32x2*>(lds + w * WLDS + wave * D + 2 * lane);
    const f32x2 ot = {ft * __uint_as_float(vtu << 16), ft * __uint_as_float(vtu & 0xffff0000u)};
    f32x2 o = (((ot + ov[0]) + ov[1]) + (ov[2] + ov[3])) + ((ov[4] + ov[5]) + (ov[6] + ov[7]));
    if (wave != (int)s) {
      __builtin_amdgcn_raw_buffer_store_b128(u32x4{__float_as_uint(o[0]), tag, __float_as_uint(o[1]), tag}, rw, lane * 16, 1024 + (wave * NS + s) * 1024, AUX_SC1);
    } else {
      u32x4 g[NS - 1];
      __builtin_amdgcn_s_sleep(6);
      while (BEFORE(tpoll)) {}
      for (unsigned spins = 0;; ++spins) {
        bool ok = true;
        asm volatile("" ::: "memory");
#pragma unroll
        for (int k = 0; k < NS - 1; ++k) g[k] = __builtin_amdgcn_raw_buffer_load_b128(rw, lane * 16, 1024 + (s * NS + ((s + 1 + k) & 3)) * 1024, AUX_SC1);
#pragma unroll
        for (int k = 0; k < NS - 1; ++k) ok &= g[k][1] == tag && g[k][3] == tag;
        if (__all(ok) || spins >= SPIN_CAP) break;
      }
#pragma unroll
      for (int k = 0; k < NS - 1; ++k) { o[0] += __uint_as_float(g[k][0]); o[1] += __uint_as_float(g[k][2]); }
      const float pv = __uint_as_float(pack_bf16(o[0], o[1]));
      const u32x4 w4 = {__float_as_uint(pv), __float_as_uint(dpp<0x55>(pv)), __float_as_uint(dpp<0xAA>(pv)), __float_as_uint(dpp<0xFF>(pv))};
      __builtin_amdgcn_raw_buffer_store_b128(w4, make_rsrc((const void*)ka[6], B_ * HQ * D * 2), (lane & 3) ? 0x40000000 : lane * 4, bid * (D * 2), AUX_SC1);
    }
  }
  asm volatile(".Lattn_code_tail:");
}
}

extern "C" void kernel_launch(void* const* d_in, const int* in_sizes, int n_in, void* d_out, int out_size, void* d_ws, size_t ws_size,
                              hipStream_t stream) {
  attn_kernel<<<NWG, NT, 0, stream>>>(d_in[0], d_in[1], d_in[2], d_in[3], d_in[4], d_in[5], d_out, d_ws);
}
```
